```python
import torch

def _softmax(x, axis=-1):
    return torch.softmax(x, dim=axis)
def _gelu(x):
    return torch.nn.functional.gelu(x, approximate="tanh")
def _silu(x):
    return torch.nn.functional.silu(x)
def _sigmoid(x):
    return torch.sigmoid(x)
def _layernorm(x, axis=-1, eps=1e-5):
    m = x.mean(dim=axis, keepdim=True)
    v = x.var(dim=axis, keepdim=True, unbiased=False)
    return (x - m) / torch.sqrt(v + eps)
def _scatter_add(tgt, idx, src, axis=0):
    return tgt.clone().index_add_(axis, idx, src)
def _segment_sum(src, seg, n):
    out = torch.zeros((n,) + src.shape[1:], dtype=src.dtype)
    return out.index_add_(0, seg, src)
def _pad(x, pw):
    flat = tuple(v for pair in reversed(pw) for v in pair)
    return torch.nn.functional.pad(x, flat)

kernel_name = 'gated_mix__sliding_block_cached__dense_activation_silu__decode'

def setup_inputs():

    _w = (lambda a: (lambda t: t.to(torch.bfloat16) if t.is_floating_point() else t)(torch.as_tensor(a)))
    def _A_setup():
        B, Sq, D = 8, 1, 448
        Hq, Hkv, Sk, Dh = 16, 8, 8192, 256
        assert Hq % Hkv == 0, (Hq, Hkv)
        return {
            "x": _w((lambda *s: torch.randn(*s, dtype=torch.bfloat16) * 0.1)(B, Sq, D)),
            "k_cache": _w((lambda *s: torch.randn(*s, dtype=torch.bfloat16) * 3.2)(B, Hkv, Sk, Dh)),
            "v_cache": _w((lambda *s: torch.randn(*s, dtype=torch.bfloat16) * 3.2)(B, Hkv, Sk, Dh)),
            "wq": _w((lambda *s: torch.randn(*s, dtype=torch.bfloat16) * 3.2)(D, Hq * Dh) * (D ** -0.5)),
            "wo": _w((lambda *s: torch.randn(*s, dtype=torch.bfloat16) * 3.2)(Hq * Dh, D) * ((Hq * Dh) ** -0.5)),
        }
    _A_in = _A_setup()

    def _B_setup():
        B, S, D, F = 8, 1, 448, 1792
        return {
            "x": _w((lambda *s: torch.randn(*s, dtype=torch.bfloat16) * 0.1)(B, S, D)),
            "w1": _w((lambda *s: torch.randn(*s, dtype=torch.bfloat16) * 3.2)(D, F) * (D ** -0.5)),
            "w2": _w((lambda *s: torch.randn(*s, dtype=torch.bfloat16) * 3.2)(F, D) * (F ** -0.5)),
        }
    _B_in = _B_setup()

    return {
        "A_x": _A_in["x"],
        "A_k_cache": _A_in["k_cache"],
        "A_v_cache": _A_in["v_cache"],
        "A_wq": _A_in["wq"],
        "A_wo": _A_in["wo"],
        "B_x": _B_in["x"],
        "B_w1": _B_in["w1"],
        "B_w2": _B_in["w2"],
        "gate_w": _w((lambda *s: torch.randn(*s, dtype=torch.bfloat16) * 0.1)(448) * 16.0),
    }

def reference(A_x, A_k_cache, A_v_cache, A_wq, A_wo, B_x, B_w1, B_w2, gate_w):
    def _A_ref(x, k_cache, v_cache, wq, wo):
        B, Sq, D = x.shape
        Hq, Dh = 16, 256
        Hkv, Sk = k_cache.shape[1], k_cache.shape[2]
        W = min(4096, max(256, Sk // 4))
        rep = Hq // Hkv
        q = (lambda x, *d: x.permute(*d))((x @ wq).reshape(B, Sq, Hq, Dh), 0, 2, 1, 3)
        k = (lambda x, n, axis: torch.repeat_interleave(x, n, dim=axis))(k_cache[:, :, Sk - W:, :], rep, axis=1)
        v = (lambda x, n, axis: torch.repeat_interleave(x, n, dim=axis))(v_cache[:, :, Sk - W:, :], rep, axis=1)
        scores = torch.einsum("bhqd,bhkd->bhqk", q, k) * (Dh ** -0.5)
        p = _softmax(scores, axis=-1)
        o = torch.einsum("bhqk,bhkd->bhqd", p, v)
        return (lambda x, *d: x.permute(*d))(o, 0, 2, 1, 3).reshape(B, Sq, Hq * Dh) @ wo

    def _B_ref(x, w1, w2):
        h = torch.einsum("bsd,df->bsf", x, w1)
        h = _silu(h)
        out = torch.einsum("bsf,fd->bsd", h, w2)
        return out

    x = A_x
    g = _sigmoid((x * gate_w).sum(-1, keepdims=True))
    a = _A_ref(x, A_k_cache, A_v_cache, A_wq, A_wo)
    b = _B_ref(x, B_w1, B_w2)
    return g * a + (1 - g) * b
```

```cpp
#include <hip/hip_runtime.h>
#include <hip/hip_bf16.h>
#include <stdint.h>

typedef unsigned v4u __attribute__((ext_vector_type(4)));
typedef float f32x4 __attribute__((ext_vector_type(4)));
typedef __attribute__((ext_vector_type(2))) __bf16 bf16x2;

constexpr int B = 8, D = 448, HQ = 16, HKV = 8, SK = 8192, DH = 256, W = 2048, F = 1792;
constexpr int NSPLIT = 4;

__device__ __forceinline__ float bflo(unsigned u) { return __builtin_bit_cast(float, u << 16); }
__device__ __forceinline__ float bfhi(unsigned u) { return __builtin_bit_cast(float, u & 0xffff0000u); }
__device__ __forceinline__ float bf2f(unsigned short u) { return __builtin_bit_cast(float, (unsigned)u << 16); }
__device__ __forceinline__ unsigned short f2bf(float f) { return __builtin_bit_cast(unsigned short, (__bf16)f); }
__device__ __forceinline__ float dot2(unsigned k, unsigned q, float acc) {
  return __builtin_amdgcn_fdot2_f32_bf16(__builtin_bit_cast(bf16x2, k), __builtin_bit_cast(bf16x2, q), acc, false);
}

__global__ void __launch_bounds__(256) k_proj(const unsigned short* x, const unsigned short* wq, const unsigned short* w1,
                                              unsigned short* q, float* h) {
  __shared__ float xs[B][D];
  __shared__ float red[32][B][64 + 1];
  const int tid = threadIdx.x;
  for (int i = tid; i < B * D; i += 256) xs[i / D][i % D] = bf2f(x[i]);
  __syncthreads();
  const int t = blockIdx.x;
  const bool isq = t < 64;
  const unsigned short* w = isq ? wq : w1;
  const int ld = isq ? HQ * DH : F;
  const int c0 = (isq ? t : t - 64) * 64;
  const int c = tid & 7, rg = tid >> 3;
  float acc[B][8];
#pragma unroll
  for (int b = 0; b < B; ++b)
#pragma unroll
    for (int j = 0; j < 8; ++j) acc[b][j] = 0.f;
  v4u wv[14];
#pragma unroll
  for (int i = 0; i < 14; ++i) wv[i] = *(const v4u*)(w + (size_t)(rg + 32 * i) * ld + c0 + 8 * c);
#pragma unroll
  for (int i = 0; i < 14; ++i) {
#pragma unroll
    for (int b = 0; b < B; ++b) {
      const float xv = xs[b][rg + 32 * i];
#pragma unroll
      for (int k = 0; k < 4; ++k) {
        acc[b][2 * k] = fmaf(xv, bflo(wv[i][k]), acc[b][2 * k]);
        acc[b][2 * k + 1] = fmaf(xv, bfhi(wv[i][k]), acc[b][2 * k + 1]);
      }
    }
  }
#pragma unroll
  for (int b = 0; b < B; ++b)
#pragma unroll
    for (int j = 0; j < 8; ++j) red[rg][b][8 * c + j] = acc[b][j];
  __syncthreads();
  for (int i = tid; i < B * 64; i += 256) {
    const int b = i >> 6, col = i & 63;
    float s = 0.f;
    for (int r = 0; r < 32; ++r) s += red[r][b][col];
    if (isq) q[b * (HQ * DH) + c0 + col] = f2bf(s);
    else {
      const float hv = bf2f(f2bf(s));
      h[b * F + c0 + col] = bf2f(f2bf(hv / (1.f + __expf(-hv))));
    }
  }
}

__global__ void __launch_bounds__(512) k_attn(const unsigned short* kc, const unsigned short* vc, const unsigned short* q,
                                              float* po, float* pml) {
  __shared__ float sp[8][2][64];
  __shared__ float so[8][2][2][DH];
  __shared__ float sml[8][2][2];
  const int wg = blockIdx.x, kvh = wg & 7, s = (wg >> 3) & 3, b = wg >> 5;
  const int tid = threadIdx.x, lane = tid & 63, w = __builtin_amdgcn_readfirstlane(tid >> 6);
  const int hf = lane >> 5, c = lane & 31;
  const size_t base = ((size_t)(b * HKV + kvh) * SK + (SK - W) + s * 512 + w * 64) * DH;
  const v4u* kp = (const v4u*)(kc + base) + lane;
  const v4u* vp = (const v4u*)(vc + base) + lane;
  const v4u q0 = *(const v4u*)(q + b * (HQ * DH) + (2 * kvh) * DH + 8 * c);
  const v4u q1 = *(const v4u*)(q + b * (HQ * DH) + (2 * kvh + 1) * DH + 8 * c);
  v4u a[32];
#pragma unroll
  for (int i = 0; i < 32; ++i) a[i] = __builtin_nontemporal_load(kp + i * 64);
  __builtin_amdgcn_sched_barrier(0);
#pragma unroll
  for (int i = 0; i < 32; ++i) {
    float s0 = 0.f, s1 = 0.f;
#pragma unroll
    for (int k = 0; k < 4; ++k) { s0 = dot2(a[i][k], q0[k], s0); s1 = dot2(a[i][k], q1[k], s1); }
    a[i] = __builtin_nontemporal_load(vp + i * 64);
#pragma unroll
    for (int o = 1; o < 32; o <<= 1) { s0 += __shfl_xor(s0, o); s1 += __shfl_xor(s1, o); }
    if (c == 0) { sp[w][0][2 * i + hf] = s0; sp[w][1][2 * i + hf] = s1; }
    __builtin_amdgcn_sched_barrier(0);
  }
  const float C2 = 0.0625f * 1.4426950408889634f;
  float x0 = sp[w][0][lane] * C2, x1 = sp[w][1][lane] * C2;
  float m0 = x0, m1 = x1;
#pragma unroll
  for (int o = 1; o < 64; o <<= 1) { m0 = fmaxf(m0, __shfl_xor(m0, o)); m1 = fmaxf(m1, __shfl_xor(m1, o)); }
  float p0 = exp2f(x0 - m0), p1 = exp2f(x1 - m1);
  float l0 = p0, l1 = p1;
#pragma unroll
  for (int o = 1; o < 64; o <<= 1) { l0 += __shfl_xor(l0, o); l1 += __shfl_xor(l1, o); }
  sp[w][0][lane] = p0; sp[w][1][lane] = p1;
  float acc0[8], acc1[8];
#pragma unroll
  for (int j = 0; j < 8; ++j) { acc0[j] = 0.f; acc1[j] = 0.f; }
#pragma unroll
  for (int i = 0; i < 32; ++i) {
    const float pa = sp[w][0][2 * i + hf], pb = sp[w][1][2 * i + hf];
#pragma unroll
    for (int k = 0; k < 4; ++k) {
      const float lo = bflo(a[i][k]), hi = bfhi(a[i][k]);
      acc0[2 * k] = fmaf(pa, lo, acc0[2 * k]); acc0[2 * k + 1] = fmaf(pa, hi, acc0[2 * k + 1]);
      acc1[2 * k] = fmaf(pb, lo, acc1[2 * k]); acc1[2 * k + 1] = fmaf(pb, hi, acc1[2 * k + 1]);
    }
  }
#pragma unroll
  for (int j = 0; j < 8; ++j) { so[w][hf][0][8 * c + j] = acc0[j]; so[w][hf][1][8 * c + j] = acc1[j]; }
  if (lane == 0) { sml[w][0][0] = m0; sml[w][0][1] = l0; sml[w][1][0] = m1; sml[w][1][1] = l1; }
  __syncthreads();
  const int g = tid >> 8, d = tid & 255;
  float M = -INFINITY;
#pragma unroll
  for (int ww = 0; ww < 8; ++ww) M = fmaxf(M, sml[ww][g][0]);
  float L = 0.f, o = 0.f;
#pragma unroll
  for (int ww = 0; ww < 8; ++ww) {
    const float sc = exp2f(sml[ww][g][0] - M);
    L = fmaf(sc, sml[ww][g][1], L);
    o = fmaf(sc, so[ww][0][g][d] + so[ww][1][g][d], o);
  }
  const int row = (b * HQ + 2 * kvh + g) * NSPLIT + s;
  po[row * DH + d] = o;
  if (d == 0) { pml[2 * row] = M; pml[2 * row + 1] = L; }
}

__global__ void __launch_bounds__(512) k_out(const unsigned short* x, const unsigned short* gw, const unsigned short* wo, const unsigned short* w2,
                                             const float* po, const float* pml, const float* h, unsigned short* out) {
  __shared__ float ov[HQ * DH];
  __shared__ float hv[F];
  __shared__ float red[2][64][64 + 1];
  __shared__ float sg;
  const int b = blockIdx.x / 7, ct = blockIdx.x % 7, tid = threadIdx.x;
  for (int i = tid; i < HQ * DH; i += 512) {
    const int hq = i >> 8, d = i & 255, row = (b * HQ + hq) * NSPLIT;
    float M = -INFINITY;
    for (int s = 0; s < NSPLIT; ++s) M = fmaxf(M, pml[2 * (row + s)]);
    float L = 0.f, o = 0.f;
    for (int s = 0; s < NSPLIT; ++s) { const float sc = exp2f(pml[2 * (row + s)] - M); L = fmaf(sc, pml[2 * (row + s) + 1], L); o = fmaf(sc, po[(row + s) * DH + d], o); }
    ov[i] = bf2f(f2bf(o / L));
  }
  for (int i = tid; i < F; i += 512) hv[i] = h[b * F + i];
  if (tid < 64) {
    float s = 0.f;
    for (int i = tid; i < D; i += 64) s += bf2f(f2bf(bf2f(x[b * D + i]) * bf2f(gw[i])));
    for (int o = 1; o < 64; o <<= 1) s += __shfl_xor(s, o);
    s = bf2f(f2bf(s));
    if (tid == 0) sg = bf2f(f2bf(1.f / (1.f + __expf(-s))));
  }
  __syncthreads();
  const int c = tid & 7, rg = tid >> 3;
  float a[8], f[8];
#pragma unroll
  for (int j = 0; j < 8; ++j) { a[j] = 0.f; f[j] = 0.f; }
#pragma unroll 16
  for (int r = rg; r < HQ * DH; r += 64) {
    const v4u wv = *(const v4u*)(wo + (size_t)r * D + ct * 64 + 8 * c);
    const float xv = ov[r];
#pragma unroll
    for (int k = 0; k < 4; ++k) { a[2 * k] = fmaf(xv, bflo(wv[k]), a[2 * k]); a[2 * k + 1] = fmaf(xv, bfhi(wv[k]), a[2 * k + 1]); }
  }
#pragma unroll 14
  for (int r = rg; r < F; r += 64) {
    const v4u wv = *(const v4u*)(w2 + (size_t)r * D + ct * 64 + 8 * c);
    const float xv = hv[r];
#pragma unroll
    for (int k = 0; k < 4; ++k) { f[2 * k] = fmaf(xv, bflo(wv[k]), f[2 * k]); f[2 * k + 1] = fmaf(xv, bfhi(wv[k]), f[2 * k + 1]); }
  }
#pragma unroll
  for (int j = 0; j < 8; ++j) { red[0][rg][8 * c + j] = a[j]; red[1][rg][8 * c + j] = f[j]; }
  __syncthreads();
  if (tid < 64) {
    float sa = 0.f, sf = 0.f;
    for (int r = 0; r < 64; ++r) { sa += red[0][r][tid]; sf += red[1][r][tid]; }
    const float g = sg;
    out[b * D + ct * 64 + tid] = f2bf(g * sa + (1.f - g) * sf);
  }
}

extern "C" void kernel_launch(void* const* d_in, const int* in_sizes, int n_in, void* d_out, int out_size, void* d_ws, size_t ws_size,
                              hipStream_t stream) {
  const unsigned short* x = (const unsigned short*)d_in[0];
  const unsigned short* kc = (const unsigned short*)d_in[1];
  const unsigned short* vc = (const unsigned short*)d_in[2];
  const unsigned short* wq = (const unsigned short*)d_in[3];
  const unsigned short* wo = (const unsigned short*)d_in[4];
  const unsigned short* w1 = (const unsigned short*)d_in[6];
  const unsigned short* w2 = (const unsigned short*)d_in[7];
  const unsigned short* gw = (const unsigned short*)d_in[8];
  char* ws = (char*)d_ws;
  unsigned short* q = (unsigned short*)ws;
  float* h = (float*)(ws + 65536);
  float* pml = (float*)(ws + 2 * 65536);
  float* po = (float*)(ws + 3 * 65536);
  k_proj<<<64 + 28, 256, 0, stream>>>(x, wq, w1, q, h);
  k_attn<<<256, 512, 0, stream>>>(kc, vc, q, po, pml);
  k_out<<<56, 512, 0, stream>>>(x, gw, wo, w2, po, pml, h, (unsigned short*)d_out);
}
```

```cpp
#include <hip/hip_runtime.h>
#include <stdint.h>

#define GAS __attribute__((address_space(1)))
#define LAS __attribute__((address_space(3)))
typedef unsigned v4u __attribute__((ext_vector_type(4)));
typedef unsigned v2u __attribute__((ext_vector_type(2)));
typedef float f32x4 __attribute__((ext_vector_type(4)));
typedef float f32x2 __attribute__((ext_vector_type(2)));
typedef short v4s __attribute__((ext_vector_type(4)));
typedef __bf16 v8bf __attribute__((ext_vector_type(8)));
typedef __bf16 v2bf __attribute__((ext_vector_type(2)));
typedef __bf16 v16bf __attribute__((ext_vector_type(16)));
typedef unsigned v8u __attribute__((ext_vector_type(8)));

namespace {

constexpr int DM = 448, SK = 8192, DH = 256, WKEYS = 2048, FF = 1792, QW = 4096;

constexpr int NTHR = 12 * 64;
constexpr int WM = 3;
constexpr int W = WM + 1;
constexpr int KC0 = 24;
constexpr int VC0 = 51;
constexpr int SB1 = 59;
constexpr int KRATE = 4, VRATE = 3;
constexpr int PACE0 = 8;
constexpr int CODE_BYTES = 22016;

enum { PA, PD0, PK0, PD1, PK1, PD2, PHI, PLO, JT, QT, HT, OT0, OT1, O2T0, O2T1, O3T0, O3T1, FT0, FT1, NG, ST, XT, TT, TD, NPRM };
#define DEFAULTS {52, 514, 19, 486, 48, 434, 12, 18, 560, 610, 610, 300, 1350, 400, 1400, 100, 900, 760, 1600, 8, 135, 225, 157, 20}
constexpr int kPrm[NPRM] = DEFAULTS;
#define PRM(k) kPrm[k]
#define DUE(i) (PRM(PA) + ((((i) < PRM(PK0) ? (i) : PRM(PK0)) * PRM(PD0) + ((i) > PRM(PK0) ? ((i) < PRM(PK1) ? (i) : PRM(PK1)) - PRM(PK0) : 0) * PRM(PD1) + ((i) > PRM(PK1) ? (i) - PRM(PK1) : 0) * PRM(PD2)) >> 4))

constexpr int WS_T = 2048;
constexpr int WS_F = 4096;
constexpr int WS_Q = 65536;
constexpr int WS_H = WS_Q + 64 * 4096;
constexpr int WS_S = 1 << 19;
constexpr int WS_P = 1 << 20;
constexpr int WS_END = WS_P + 112 * 4096;
static_assert(WS_F + 64 * 512 <= WS_Q && WS_H + 8 * 14336 <= WS_S && WS_S + 64 * 8192 <= WS_P, "workspace");
constexpr int OOB = 0x7ffffff0;

constexpr int ROWP = 928;
constexpr int L_WO = 0;
constexpr int L_X = 128 * ROWP;
constexpr int L_SC = L_X + 4 * ROWP;
constexpr int L_PT = L_SC + 8 * 2112;
constexpr int L_FF = L_PT + 8 * 1152;
constexpr int L_OP = L_SC;
constexpr int L_QF = L_FF + 2 * DM * 4;
constexpr int L_HB = L_QF + 1024;
constexpr int L_GX = L_HB + 512;
constexpr int L_EP = L_GX + 2 * 896;
constexpr int L_ST = L_EP + 512;
constexpr int SIG_TAG = L_ST + 64, SIG_JOB = SIG_TAG + 4, SIG_Q = SIG_TAG + 8, SIG_SC = SIG_TAG + 12, SIG_P = SIG_TAG + 16, SIG_W = SIG_TAG + 20, SIG_EP = SIG_TAG + 24;
constexpr int L_END = L_ST + 128;
static_assert(L_END <= 163840, "LDS");

constexpr int AUX_SC1 = 16, AUX_NT = 2;
constexpr unsigned SPIN_CAP = 1u << 18;

template <int I, int N, class F> __device__ __forceinline__ void static_for(F&& f) {
  if constexpr (I < N) { f(std::integral_constant<int, I>{}); static_for<I + 1, N>(f); }
}
constexpr int WAITVM(int n) { return (n & 15) | ((n >> 4) << 14) | (7 << 4) | (15 << 8); }
#define WAIT_VM(n) __builtin_amdgcn_s_waitcnt(WAITVM(n))
#define WAIT_LGKM0() __builtin_amdgcn_s_waitcnt(0xC07F)
#define DRAIN_VM() asm volatile("s_waitcnt vmcnt(0)" ::: "memory")
#define SCHED() __builtin_amdgcn_sched_barrier(0)
#define CFENCE() asm volatile("" ::: "memory")
__device__ __forceinline__ void wg_barrier() { CFENCE(); WAIT_LGKM0(); __builtin_amdgcn_s_barrier(); CFENCE(); }

__device__ __forceinline__ float u2f(unsigned u) { return __builtin_bit_cast(float, u); }
__device__ __forceinline__ unsigned f2u(float f) { return __builtin_bit_cast(unsigned, f); }
__device__ __forceinline__ float bflo(unsigned u) { return u2f(u << 16); }
__device__ __forceinline__ float bfhi(unsigned u) { return u2f(u & 0xffff0000u); }
__device__ __forceinline__ unsigned pkbf(float lo, float hi) { v2bf r = {(__bf16)lo, (__bf16)hi}; return __builtin_bit_cast(unsigned, r); }
__device__ __forceinline__ unsigned addbf(unsigned a, unsigned b) { return pkbf(bflo(a) + bflo(b), bfhi(a) + bfhi(b)); }
template <int CTRL> __device__ __forceinline__ float dpp(float x) {
  return u2f((unsigned)__builtin_amdgcn_mov_dpp((int)f2u(x), CTRL, 0xf, 0xf, true));
}
constexpr int XOR1 = 0xB1, XOR2 = 0x4E, HMIR = 0x141, MIR = 0x140, ROR = 0x120;
__device__ __forceinline__ float sw16_add(float x) { auto s = __builtin_amdgcn_permlane16_swap(f2u(x), f2u(x), false, false); return u2f(s[0]) + u2f(s[1]); }
__device__ __forceinline__ float sw32_add(float a, float b) { auto s = __builtin_amdgcn_permlane32_swap(f2u(a), f2u(b), false, false); return u2f(s[0]) + u2f(s[1]); }
__device__ __forceinline__ float wave_sum(float x) {
  x += dpp<XOR1>(x); x += dpp<XOR2>(x); x += dpp<HMIR>(x); x += dpp<MIR>(x);
  x = sw16_add(x); return sw32_add(x, x);
}
__device__ __forceinline__ float sum4(f32x4 d) { const f32x2 t = d.xy + d.zw; return t.x + t.y; }
__device__ __forceinline__ unsigned rlu(unsigned x, int l) { return (unsigned)__builtin_amdgcn_readlane((int)x, l); }
__device__ __forceinline__ float rl(float x, int l) { return u2f(rlu(f2u(x), l)); }
__device__ __forceinline__ void wave_max2(float& a, float& b) {
  auto t = __builtin_amdgcn_permlane32_swap(f2u(a), f2u(b), false, false);
  float x = fmaxf(u2f(t[0]), u2f(t[1]));
  x = fmaxf(x, dpp<XOR1>(x)); x = fmaxf(x, dpp<XOR2>(x)); x = fmaxf(x, dpp<HMIR>(x)); x = fmaxf(x, dpp<MIR>(x));
  a = fmaxf(rl(x, 0), rl(x, 16)); b = fmaxf(rl(x, 32), rl(x, 48));
}
__device__ __forceinline__ void wave_sum2(float& a, float& b) {
  float x = sw32_add(a, b);
  x += dpp<XOR1>(x); x += dpp<XOR2>(x); x += dpp<HMIR>(x); x += dpp<MIR>(x);
  a = rl(x, 0) + rl(x, 16); b = rl(x, 32) + rl(x, 48);
}
__device__ __forceinline__ f32x4 mfma32(v4u a, v4u b, f32x4 c) {
  return __builtin_amdgcn_mfma_f32_16x16x32_bf16(__builtin_bit_cast(v8bf, a), __builtin_bit_cast(v8bf, b), c, 0, 0, 0);
}
__device__ __forceinline__ f32x4 smfmac64(v4u a, v8u b, f32x4 c, int idx) {
  return __builtin_amdgcn_smfmac_f32_16x16x64_bf16(__builtin_bit_cast(v8bf, a), __builtin_bit_cast(v16bf, b), c, idx, 0, 0);
}
__device__ __forceinline__ v2u tr_read(LAS char* p) { return __builtin_bit_cast(v2u, __builtin_amdgcn_ds_read_tr16_b64_v4i16((LAS v4s*)p)); }
__device__ __forceinline__ __amdgpu_buffer_rsrc_t mkrs(const char* p, int bytes) { return __builtin_amdgcn_make_buffer_rsrc((void*)p, 0, bytes, 0x00020000); }
__device__ __forceinline__ v4u ld_sc1(__amdgpu_buffer_rsrc_t rs, int off) { return __builtin_amdgcn_raw_buffer_load_b128(rs, off, 0, AUX_SC1); }
__device__ __forceinline__ void st_sc1(__amdgpu_buffer_rsrc_t rs, int off, v4u v) { __builtin_amdgcn_raw_buffer_store_b128(v, rs, off, 0, AUX_SC1); }
__device__ __forceinline__ bool tags_ok(v4u v, unsigned ta, unsigned tb) { return v.y == ta && v.w == tb; }
__device__ __forceinline__ void dma16(const GAS char* g, LAS char* l) { __builtin_amdgcn_global_load_lds((const GAS void*)g, (LAS void*)l, 16, 0, 0); }
#define DMA_ASM(op, g, lds_off) do { unsigned keep_; \
    asm volatile("s_mov_b32 %0, m0\n\ts_mov_b32 m0, %2\n\ts_nop 0\n\t" op "\n\ts_mov_b32 m0, %0" : "=&s"(keep_) : "v"(g), "s"(lds_off) : "memory"); } while (0)
__device__ __forceinline__ void dma16_asm(const GAS char* g, unsigned l) { DMA_ASM("global_load_lds_dwordx4 %1, off", g, l); }
__device__ __forceinline__ void dma16_nt_asm(const GAS char* g, unsigned l) { DMA_ASM("global_load_lds_dwordx4 %1, off nt", g, l); }
__device__ __forceinline__ void dma4_asm(const GAS char* g, unsigned l) { DMA_ASM("global_load_lds_dword %1, off", g, l); }
__device__ __forceinline__ void dma4_sc1_asm(const GAS char* g, unsigned l) { DMA_ASM("global_load_lds_dword %1, off sc1", g, l); }
struct Tags { unsigned a, b, epoch; };
__device__ __forceinline__ Tags make_tags(LAS char* ep) {
  Tags t;
  t.epoch = __builtin_amdgcn_readfirstlane(*(LAS unsigned*)ep);
  t.a = (t.epoch ^ 0x9E3779B9u) * 0x85EBCA6Bu; t.a ^= t.a >> 13; t.a *= 0xC2B2AE35u; t.a ^= t.a >> 16;
  t.b = (t.epoch ^ 0x7F4A7C15u) * 0x2C1B3C6Du; t.b ^= t.b >> 12; t.b *= 0x297A2D39u; t.b ^= t.b >> 15;
  return t;
}
struct Sel { unsigned lo[4], hi[4]; };
__device__ __forceinline__ Sel make_sel(int lane) {
  Sel s;
#pragma unroll
  for (int r = 0; r < 4; ++r) {
    const bool mine = ((lane & 7) >> 1) == r;
    s.lo[r] = mine ? ((lane & 1) ? 0x01000c0cu : 0x0c0c0100u) : 0x0c0c0c0cu;
    s.hi[r] = mine ? ((lane & 1) ? 0x03020c0cu : 0x0c0c0302u) : 0x0c0c0c0cu;
  }
  return s;
}
__device__ __forceinline__ v4u place(unsigned x, const unsigned (&sel)[4]) {
  return v4u{__builtin_amdgcn_perm(x, x, sel[0]), __builtin_amdgcn_perm(x, x, sel[1]), __builtin_amdgcn_perm(x, x, sel[2]), __builtin_amdgcn_perm(x, x, sel[3])};
}
typedef unsigned short v2h __attribute__((ext_vector_type(2)));
struct Weights { unsigned a[8], b[8]; };
__device__ __forceinline__ Weights make_weights(int lane) {
  Weights w;
#pragma unroll
  for (int k = 0; k < 8; ++k) {
    unsigned t = (unsigned)(lane * 8 + k + 1) * 0x9E3779B1u; t ^= t >> 15; w.a[k] = t | 0x00010001u;
    t *= 0x85EBCA77u; t ^= t >> 13; w.b[k] = t | 0x00010001u;
  }
  return w;
}
__device__ __forceinline__ unsigned checksum(v4u p, v4u q, const Weights& w) {
  unsigned a = 0, b = 0;
#pragma unroll
  for (int k = 0; k < 8; ++k) {
    const unsigned x = k < 4 ? p[k & 3] : q[k & 3];
    a = __builtin_amdgcn_udot2(__builtin_bit_cast(v2h, x), __builtin_bit_cast(v2h, w.a[k]), a, false);
    b = __builtin_amdgcn_udot2(__builtin_bit_cast(v2h, x), __builtin_bit_cast(v2h, w.b[k]), b, false);
  }
  auto t = __builtin_amdgcn_permlane32_swap(a, b, false, false);
  unsigned x = t[0] + t[1];
  x += __builtin_amdgcn_mov_dpp(x, XOR1, 0xf, 0xf, true); x += __builtin_amdgcn_mov_dpp(x, XOR2, 0xf, 0xf, true);
  x += __builtin_amdgcn_mov_dpp(x, HMIR, 0xf, 0xf, true); x += __builtin_amdgcn_mov_dpp(x, MIR, 0xf, 0xf, true);
  return x;
}
template <int NAP = 1> __device__ __forceinline__ void not_before(unsigned t) {
  while ((int)((unsigned)__builtin_amdgcn_s_memrealtime() - t) < 0) { if (NAP) __builtin_amdgcn_s_sleep(NAP); }
}
__device__ __forceinline__ unsigned clock_origin(LAS char* L) { return __builtin_amdgcn_readfirstlane(*(volatile LAS unsigned*)(L + L_ST + 48)); }
__device__ __forceinline__ void lds_signal(LAS char* p, int lane) {
  CFENCE(); WAIT_LGKM0();
  if (lane == 0) asm volatile("ds_add_u32 %0, %1" ::"v"((unsigned)(size_t)p), "v"(1u) : "memory");
}
__device__ __forceinline__ void lds_await(LAS char* p, unsigned want) {
  if (__builtin_expect(__builtin_amdgcn_readfirstlane(*(volatile LAS unsigned*)p) < want, 0)) {
    do { __builtin_amdgcn_s_sleep(1); } while (__builtin_amdgcn_readfirstlane(*(volatile LAS unsigned*)p) < want);
  }
  CFENCE();
}

constexpr int kdone(int i) { if (i < KC0) return 0; int a = (i - KC0 + 1) * KRATE, b = i - W + 1; a = a < b ? a : b; return a < 32 ? a : 32; }
constexpr int vdone(int i) { if (i < VC0) return 0; int a = (i - VC0 + 1) * VRATE, b = i - W + 1 - 32; a = a < b ? a : b; return a < 32 ? a : 32; }
constexpr int kend() { int i = KC0; while (kdone(i) < 32) ++i; return i; }
static_assert(vdone(63 + W) == 32 && vdone(62 + W) == 31, "V catches up before its last load lands");
static_assert(VC0 > kend() && SB1 > VC0 && SB1 + 3 < 63 + W, "schedule order");

struct Args {
  const char *kc, *vc;
  char* ws;
  const char *x, *wq, *w1, *w2, *wo, *gw;
  unsigned short* out;
};

__global__ void __launch_bounds__(NTHR, 3) fused_decode(Args a) {
  __shared__ __attribute__((aligned(16))) char smem[L_END];
  LAS char* L = (LAS char*)smem;
  const int wg = blockIdx.x, h = wg & 7, s = (wg >> 3) & 3, b = wg >> 5;
  const int lane = threadIdx.x & 63, wave = __builtin_amdgcn_readfirstlane(threadIdx.x >> 6);
  const int w = wave & 7, bh = b * 8 + h;
  const unsigned l0 = (unsigned)(size_t)L;
  if (wave == 10) {
    unsigned long long pc;
    asm volatile("s_getpc_b64 %0" : "=s"(pc));
    const int o = ((wg >> 3) + (lane & 7) * 32) * 128;
    dma4_asm((const GAS char*)(pc & ~127ull) + (o < CODE_BYTES ? o : 0), l0 + L_EP + 256);
  }
  const size_t base = ((size_t)bh * SK + (SK - WKEYS)) * (DH * 2);
  const int kl = w * 1024 + ((lane >> 3) & 1) * 512 + (lane >> 4) * 128 + (lane & 7) * 16;
  int kst[4];
#pragma unroll
  for (int k = 0; k < 4; ++k) kst[k] = kl + ((s + k) & 3) * 8192;
  asm volatile("" : "+v"(kst[0]), "+v"(kst[1]), "+v"(kst[2]), "+v"(kst[3]));
  SCHED();
  asm volatile("" ::"s"(a.kc), "s"(a.vc), "s"(a.ws), "s"(a.x), "s"(a.wq), "s"(a.w1), "s"(a.w2), "s"(a.wo), "s"(a.gw), "s"(a.out));
  const __amdgpu_buffer_rsrc_t rk = mkrs(a.kc + base, WKEYS * DH * 2), rs = mkrs(a.ws, WS_END);
  v4u kv[64];
  if (wave < 8) {
    static_for<0, WM>([&](auto ic) {
      constexpr int I = ic.value;
      kv[I] = __builtin_amdgcn_raw_buffer_load_b128(rk, kst[I & 3], I * 32768, AUX_NT);
      SCHED();
    });
  }
  const unsigned tstart = (unsigned)__builtin_amdgcn_s_memrealtime();
  if (threadIdx.x < 12) *(LAS unsigned*)(L + L_ST + 48 + threadIdx.x * 4) = threadIdx.x < 4 ? tstart : 0u;
  wg_barrier();

  const bool job = s < 2 || (s == 2 && b < 7);
  const int hj = h ^ 1, jf = 7 * hj + b;
  const int kh = s < 2 ? s : jf & 1;
  if (wave >= 8) {
    if (w == 3) dma4_sc1_asm((const GAS char*)a.ws, l0 + L_EP);
    if (job) {
      const int ld = s < 2 ? QW * 2 : FF * 2;
      const GAS char* p = (s < 2 ? (const GAS char*)a.wq + hj * 1024 + b * 128 : (const GAS char*)a.w1 + (jf >> 1) * 128) + (kh * 224 + w * 56 + (lane >> 3)) * ld + (lane & 7) * 16;
#pragma nounroll
      for (int k = 0; k < 7; ++k) {
        dma16_nt_asm(p + k * 8 * ld, l0 + L_WO + (w * 7 + k) * 1024);
      }
    }
    if (lane < 56) {
      if (job) dma16_asm((const GAS char*)a.x + (2 * w + (lane >= 28)) * (DM * 2) + kh * 448 + (lane >= 28 ? lane - 28 : lane) * 16, l0 + L_X + w * ROWP);
      if (w == 1) {
        dma16_asm((const GAS char*)a.x + b * (DM * 2) + lane * 16, l0 + L_GX);
        dma16_asm((const GAS char*)a.gw + lane * 16, l0 + L_GX + 896);
      }
    }
  }
  int role = wave;
  asm volatile("" : "+s"(role));

  if (__builtin_expect(role < 8, 1)) {
    const __amdgpu_buffer_rsrc_t rv = mkrs(a.vc + base, WKEYS * DH * 2);
    const int vl = (lane >> 3) * 512 + (lane & 7) * 16 + s * 128;
    v4u qf, pq;
    unsigned ta = 0;
    float S = 0.f;
    Sel sel;
    v4u a0, a1;
    f32x4 acc0 = {0.f, 0.f, 0.f, 0.f}, acc1 = {0.f, 0.f, 0.f, 0.f};
    float scl = 0.f;
    f32x4 d[4];
    v8u wf[2][4];
    int opw = 0, opr = 0, pst = 0, idx = 0;
    const int bank = (lane >> 2) & 3;

    auto prep = [&](auto jc) {
      constexpr int j = jc.value;
      if constexpr (j < 32) {
        if constexpr ((j & 3) == 0) pq = *(LAS v4u*)(L + L_PT + w * 1152 + (lane >> 3) * 144 + j * 4);
        const unsigned pp = pq[j & 3];
        a0 = place(pp, sel.lo); a1 = place(pp, sel.hi);
        asm volatile("" : "+v"(a0), "+v"(a1));
      }
    };

    int lag = 0;
    unsigned origin = tstart;
    static_for<WM, 64 + W>([&](auto ic) {
      constexpr int I = ic.value;
      if constexpr (I >= W) WAIT_VM(I <= 64 ? W - 1 : 63 + W - I);
      if constexpr (I <= PACE0) WAIT_VM(WM - 1);
      else if constexpr (I < 64) { if (lag <= PRM(PHI)) { WAIT_VM(WM - 1); if (lag < -PRM(PLO)) WAIT_VM(WM - 2); } }
      SCHED();
      if constexpr (I < 32) kv[I] = __builtin_amdgcn_raw_buffer_load_b128(rk, kst[I & 3], I * 32768, AUX_NT);
      else if constexpr (I < 64) kv[I] = __builtin_amdgcn_raw_buffer_load_b128(rv, vl + ((w + 2 * I) & 7) * 4096, (I - 32) * 32768, AUX_NT);
      SCHED();
      unsigned now = 0;
      if constexpr (I >= PACE0 && I < 63) {
        now = (unsigned)__builtin_amdgcn_s_memrealtime();
        if constexpr ((I & 1) == 0 && I <= 24) origin = *(volatile LAS unsigned*)(L + L_ST + 48);
      }
      if constexpr (I == KC0) {
        lds_await(L + SIG_Q, 1u);
        qf = *(LAS v4u*)(L + L_QF + ((lane & 1) * 32 + (lane >> 4) * 8 + ((lane >> 1) & 1) * 4 + bank) * 16);
      }
      static_for<kdone(I - 1), kdone(I)>([&](auto jc) {
        constexpr int j = jc.value, t = j & 3;
        const f32x4 d = mfma32(kv[j], qf, f32x4{0.f, 0.f, 0.f, 0.f});
        float z = d[t] + dpp<ROR + 4>(d[(t + 3) & 3]);
        z += dpp<ROR + 8>(d[(t + 2) & 3]);
        z += dpp<ROR + 12>(d[(t + 1) & 3]);
        if constexpr (t == 0) S = z; else S = (bank == t) ? z : S;
        if constexpr (t == 3) *(LAS float*)(L + L_SC + w * 2112 + (j >> 2) * 264 + lane * 4) = S;
      });
      if constexpr (I == kend()) { lds_signal(L + SIG_SC, lane); }
      if constexpr (I == VC0) {
        lds_await(L + SIG_P, 1u);
        sel = make_sel(lane);
        prep(std::integral_constant<int, 0>{});
      }
      static_for<vdone(I - 1), vdone(I)>([&](auto jc) {
        constexpr int j = jc.value;
        acc0 = mfma32(a0, kv[32 + j], acc0);
        acc1 = mfma32(a1, kv[32 + j], acc1);
        prep(std::integral_constant<int, j + 1>{});
      });
      if constexpr (I == SB1) {
        lds_await(L + SIG_W, 4u);
        const float g = *(LAS float*)(L + L_ST + 32);
        const f32x2 f = *(LAS f32x2*)(L + L_ST + (w >> 1) * 8);
        const Tags t = make_tags(L + L_EP);
        ta = (lane & 1) ? t.b : t.a;
        scl = g * (lane < 32 ? f.x : f.y);
        const int wc = w < 7 ? w : 6;
        LAS char* fp = L + L_FF + (((lane >> 4) & 1) * DM + 64 * wc + (lane & 15)) * 4;
        const float gf = lane < 32 ? 1.f - g : 0.f;
#pragma unroll
        for (int t = 0; t < 4; ++t) d[t] = f32x4{*(LAS float*)(fp + t * 64) * gf, 0.f, 0.f, 0.f};
        {
          const int c = lane & 7, a4 = (lane >> 4) & 1;
          opw = (lane & 8) ? L_PT + w * 1152 + lane * 4 : L_OP + w * 288 + (lane >> 5) * 64 + (8 * (c >> 2) + 4 * ((c >> 1) & 1) + 2 * a4 + (c & 1)) * 4;
          opr = L_OP + (lane & 15) * 144 + (lane >> 4) * 16;
          idx = (lane & 1) ? 0xEEEE : 0x4444;
          pst = (lane & 16) ? OOB : WS_P + (b * 14 + 2 * wc + (lane >> 5)) * 4096 + (h * 4 + s) * 128 + (lane & 15) * 8;
        }
        asm volatile("" : "+v"(scl), "+v"(idx), "+v"(d[0]), "+v"(d[1]), "+v"(d[2]), "+v"(d[3]), "+v"(ta), "+v"(opw), "+v"(opr), "+v"(pst));
      }
      if constexpr (I >= SB1 && I < SB1 + 4) {
        constexpr int ks = I - SB1;
        const int wc = w < 7 ? w : 6, g4 = lane >> 4;
        LAS char* rp = L + L_WO + (32 * ks + 16 * (g4 >> 1) + 4 * (g4 & 1) + ((lane >> 2) & 3)) * ROWP + wc * 128 + (lane & 3) * 8;
#pragma unroll
        for (int t = 0; t < 4; ++t) {
          const v2u lo = tr_read(rp + t * 32), hi = tr_read(rp + t * 32 + 8 * ROWP);
          if constexpr (ks & 1) wf[ks >> 1][t].hi = v4u{lo.x, lo.y, hi.x, hi.y}; else wf[ks >> 1][t].lo = v4u{lo.x, lo.y, hi.x, hi.y};
        }
      }
      SCHED();
      if constexpr (I >= PACE0 && I < 63) lag = (int)(now - __builtin_amdgcn_readfirstlane(origin)) - DUE(I);
      SCHED();
    });

    {
      f32x4 y;
#pragma unroll
      for (int r = 0; r < 4; ++r) {
        const float p0 = acc0[r], p1 = acc1[r];
        auto t = __builtin_amdgcn_permlane32_swap(f2u(p0), f2u(p1), false, false);
        y[r] = u2f(t[0]) + dpp<ROR + 8>(u2f(t[1]));
      }
      y *= scl;
      *(LAS unsigned*)(L + opw) = pkbf(y[0], y[1]);
      *(LAS unsigned*)(L + opw + 144) = pkbf(y[2], y[3]);
    }
    wg_barrier();
    if (w < 7) {
      const v4u af0 = *(LAS v4u*)(L + opr), af1 = *(LAS v4u*)(L + opr + 64);
#pragma unroll
      for (int t = 0; t < 4; ++t) d[t] = smfmac64(af0, wf[0][t], d[t], idx);
#pragma unroll
      for (int t = 0; t < 4; ++t) d[t] = smfmac64(af1, wf[1][t], d[t], idx);
      SCHED();
      const float ya = sw16_add(sw32_add(sum4(d[0]), sum4(d[2]))), yb = sw16_add(sw32_add(sum4(d[1]), sum4(d[3])));
      __builtin_amdgcn_raw_buffer_store_b64(v2u{pkbf(ya, yb), ta}, rs, pst, 0, AUX_SC1);
    }
  } else {
    const int v = w;
    if (v == 3) {
      __builtin_amdgcn_s_setprio(2);
      DRAIN_VM();
      lds_signal(L + SIG_TAG, lane);
      lds_signal(L + SIG_EP, lane);
      const Tags tg = make_tags(L + L_EP);
      const unsigned ta = tg.a, tb = tg.b;
      not_before(tstart + PRM(QT) - 60);
      not_before(clock_origin(L) + PRM(QT));
      {
        const int qoff = WS_Q + (h * 8 + b) * 4096 + lane * 16;
        v4u g[4];
        bool ok[4] = {false, false, false, false};
#pragma nounroll
        for (unsigned spin = 0; spin < SPIN_CAP; ++spin) {
          CFENCE();
          v4u t[4];
#pragma unroll
          for (int k = 0; k < 4; ++k) t[k] = ld_sc1(rs, ok[k] ? OOB : qoff + k * 1024);
          SCHED();
          bool all = true;
#pragma unroll
          for (int k = 0; k < 4; ++k) { g[k] = ok[k] ? g[k] : t[k]; ok[k] = ok[k] | tags_ok(t[k], ta, tb); all = all & ok[k]; }
          if (__all(all)) break;
          __builtin_amdgcn_s_sleep(2);
        }
#pragma unroll
        for (int k = 0; k < 2; ++k) *(LAS v2u*)(L + L_QF + k * 512 + lane * 8) = v2u{addbf(g[k].x, g[k + 2].x), addbf(g[k].z, g[k + 2].z)};
        lds_signal(L + SIG_Q, lane);
      }
      SCHED();
      Weights wt = make_weights(lane);
      const int src = lane < 8 ? lane >> 1 : (s + 1 + lane) & 3;
      const bool other = lane < 11 && src != s;
      int poff = WS_S + (bh) * 8192 + lane * 16;
      int fst = lane < 3 ? WS_F + (bh) * 512 + s * 128 + lane * 16 : OOB;
      int fld = other ? WS_F + (bh) * 512 + src * 128 + (lane < 8 ? lane & 1 : 2) * 16 : OOB;
      int pt = L_PT + (lane >> 3) * 144 + (lane & 7) * 16;
      int sp = L_SC + (lane >> 4) * 2112 + (lane & 7) * 264 + ((lane >> 3) & 1) * 128;
#pragma unroll
      for (int k = 0; k < 8; ++k) asm volatile("" : "+v"(wt.a[k]), "+v"(wt.b[k]));
      asm volatile("" : "+v"(poff), "+v"(fst), "+v"(fld), "+v"(pt), "+v"(sp));
      lds_await(L + SIG_SC, 8u);
      float m0, m1, l0 = 0.f, l1 = 0.f;
      {
        constexpr float C2 = 0.0625f * 1.4426950408889634f;
        f32x2 u0[8], u1[8];
#pragma unroll
        for (int k = 0; k < 8; ++k) {
          u0[k] = *(LAS f32x2*)(L + sp + (k >> 2) * (4 * 2112) + (k & 3) * 16); u1[k] = *(LAS f32x2*)(L + sp + (k >> 2) * (4 * 2112) + (k & 3) * 16 + 72);
        }
        SCHED();
        float x0[8], x1[8];
#pragma unroll
        for (int k = 0; k < 8; ++k) { const f32x2 t = u0[k] + u1[k]; x0[k] = t.x; x1[k] = t.y; }
        m0 = x0[0]; m1 = x1[0];
#pragma unroll
        for (int k = 1; k < 8; ++k) { m0 = fmaxf(m0, x0[k]); m1 = fmaxf(m1, x1[k]); }
        wave_max2(m0, m1);
        m0 *= C2; m1 *= C2;
        unsigned pp[8];
#pragma unroll
        for (int k = 0; k < 8; ++k) {
          const float p0 = __builtin_amdgcn_exp2f(fmaf(x0[k], C2, -m0)), p1 = __builtin_amdgcn_exp2f(fmaf(x1[k], C2, -m1));
          l0 += p0; l1 += p1; pp[k] = pkbf(p0, p1);
        }
        const v4u pa = {pp[0], pp[1], pp[2], pp[3]}, pb = {pp[4], pp[5], pp[6], pp[7]};
        const v4u xa = pa ^ ta, xb = pb ^ ta;
        const unsigned ck = checksum(xa, xb, wt);
        wave_sum2(l0, l1);
        asm volatile("" ::"v"(ck), "v"(l0), "v"(l1));
        not_before(clock_origin(L) + DUE(kend()) + PRM(ST));
        st_sc1(rs, poff + s * 2048, xa); st_sc1(rs, poff + s * 2048 + 1024, xb);
        st_sc1(rs, fst, v4u{lane == 2 ? rlu(ck, 0) + rlu(ck, 16) : f2u(lane ? m1 : m0), ta, lane == 2 ? rlu(ck, 32) + rlu(ck, 48) : f2u(lane ? l1 : l0), tb});
        *(LAS v4u*)(L + pt + s * 2304) = pa; *(LAS v4u*)(L + pt + s * 2304 + 1152) = pb;
      }
      {
        v4u fl;
        unsigned miss = 7u;
        not_before(clock_origin(L) + DUE(kend()) + PRM(XT));
#pragma nounroll
        for (unsigned spin = 0; spin < SPIN_CAP; ++spin) {
          CFENCE();
          v4u pv[6];
          fl = ld_sc1(rs, fld);
#pragma unroll
          for (int k = 0; k < 6; ++k) pv[k] = ld_sc1(rs, ((miss >> (k >> 1)) & 1) ? poff + ((s + 1 + (k >> 1)) & 3) * 2048 + (k & 1) * 1024 : OOB);
          SCHED();
          unsigned ck[3];
#pragma unroll
          for (int k = 0; k < 3; ++k) ck[k] = checksum(pv[2 * k], pv[2 * k + 1], wt);
#pragma unroll
          for (int k = 0; k < 3; ++k) if ((miss >> k) & 1) {
            *(LAS v4u*)(L + pt + ((s + 1 + k) & 3) * 2304) = pv[2 * k] ^ ta; *(LAS v4u*)(L + pt + ((s + 1 + k) & 3) * 2304 + 1152) = pv[2 * k + 1] ^ ta;
          }
          const unsigned stale = (unsigned)__ballot(other && !tags_ok(fl, ta, tb));
#pragma unroll
          for (int k = 0; k < 3; ++k) {
            const bool good = !((stale >> (8 + k)) & 1) & (rlu(ck[k], 0) + rlu(ck[k], 16) == rlu(fl.x, 8 + k)) & (rlu(ck[k], 32) + rlu(ck[k], 48) == rlu(fl.z, 8 + k));
            miss &= ~((unsigned)good << k);
          }
          if ((miss | stale) == 0u) break;
        }
        lds_signal(L + SIG_P, lane);
        const float m = other && lane < 8 ? u2f(fl.x) : ((lane & 1) ? m1 : m0), l = other && lane < 8 ? u2f(fl.z) : ((lane & 1) ? l1 : l0);
        float M = fmaxf(m, dpp<XOR2>(m)); M = fmaxf(M, dpp<XOR1>(dpp<HMIR>(M)));
        const float f = __builtin_amdgcn_exp2f(m - M);
        float Ls = f * l; Ls += dpp<XOR2>(Ls); Ls += dpp<XOR1>(dpp<HMIR>(Ls));
        if (lane < 8) *(LAS float*)(L + L_ST + lane * 4) = f / Ls;
        lds_signal(L + SIG_W, lane);
      }
      const bool keeper = b == 0 && s == 2 && h == 0;
      if (keeper) {
        const int off = lane < 56 ? WS_F + (8 + lane) * 512 + 3 * 128 : WS_F + (lane) * 512 + 2 * 128;
        bool ok = false;
#pragma nounroll
        for (unsigned spin = 0; spin < SPIN_CAP; ++spin) {
          CFENCE();
          __builtin_amdgcn_s_sleep(32);
          const v4u fl = ld_sc1(rs, ok ? OOB : off);
          ok = ok | tags_ok(fl, ta, tb);
          if (__all(ok)) break;
        }
      }
      const int c = (3 - s) * 8 + h;
      const int cc = c < 14 ? c : 0;
      int off = WS_P + (b * 14 + cc) * 4096 + lane * 16;
      int oo = (b * DM + 32 * cc + (lane & 16) + 2 * (lane & 7)) * 2;
      const unsigned one = (lane & 4) ? 0x3F800000u : 0x00003F80u, ev = (lane & 11) == 0 ? one : 0u, od = (lane & 11) == 1 ? one : 0u;
      v4u selr = {ev, ev, od, od};
      asm volatile("" : "+v"(off), "+v"(oo), "+v"(selr));
      wg_barrier();
      const unsigned own = (unsigned)__builtin_amdgcn_s_memrealtime() + PRM(TD);
      const unsigned fin = clock_origin(L) + DUE(63) + PRM(TT);
      if (c < 14) {
        not_before<0>((int)(fin - own) > 0 ? fin : own);
        unsigned long long m0, m1, m2, m3;
        unsigned left = SPIN_CAP;
        asm volatile(
            "s_mov_b64 %0, -1\n\ts_mov_b64 %1, -1\n\ts_mov_b64 %2, -1\n\ts_mov_b64 %3, -1\n"
            "1:\n\t"
            "s_mov_b64 exec, %0\n\tglobal_load_dwordx4 v[64:67], %5, %8 sc1\n\t"
            "s_mov_b64 exec, %1\n\tglobal_load_dwordx4 v[68:71], %5, %8 offset:1024 sc1\n\t"
            "s_mov_b64 exec, %2\n\tglobal_load_dwordx4 v[72:75], %5, %8 offset:2048 sc1\n\t"
            "s_mov_b64 exec, %3\n\tglobal_load_dwordx4 v[76:79], %5, %8 offset:3072 sc1\n\t"
            "s_mov_b64 exec, -1\n\t"
            "s_waitcnt vmcnt(0)\n\t"
            "v_cmp_ne_u32 %0, %10, v65\n\tv_cmp_ne_u32 vcc, %11, v67\n\tv_cmp_ne_u32 %1, %10, v69\n\ts_or_b64 %0, %0, vcc\n\t"
            "v_cmp_ne_u32 vcc, %11, v71\n\tv_cmp_ne_u32 %2, %10, v73\n\ts_or_b64 %1, %1, vcc\n\t"
            "v_cmp_ne_u32 vcc, %11, v75\n\tv_cmp_ne_u32 %3, %10, v77\n\ts_or_b64 %2, %2, vcc\n\t"
            "v_cmp_ne_u32 vcc, %11, v79\n\ts_or_b64 %3, %3, vcc\n\t"
            "s_or_b64 vcc, %0, %1\n\ts_or_b64 vcc, vcc, %2\n\ts_or_b64 vcc, vcc, %3\n\t"
            "s_cbranch_scc0 2f\n\t"
            "s_sub_u32 %4, %4, 1\n\t"
            "s_cbranch_scc0 1b\n"
            "2:\n\t"
            "v_mov_b32 v65, v68\n\tv_mov_b32 v67, v70\n\tv_mov_b32 v73, v76\n\tv_mov_b32 v75, v78\n\t"
            "v_mfma_f32_16x16x32_bf16 v[80:83], %7, v[64:67], 0\n\t"
            "s_nop 1\n\t"
            "v_mfma_f32_16x16x32_bf16 v[80:83], %7, v[72:75], v[80:83]\n\t"
            "s_nop 7\n\t"
            "v_add_f32_dpp v84, v80, v80 row_ror:8 row_mask:0xf bank_mask:0xf\n\t"
            "v_add_f32_dpp v85, v81, v81 row_ror:8 row_mask:0xf bank_mask:0xf\n\t"
            "v_cvt_pk_bf16_f32 v84, v84, v85\n\t"
            "s_mov_b64 exec, 0xff00ff\n\t"
            "global_store_dword %6, v84, %9 sc1\n\t"
            "s_mov_b64 exec, -1"
            : "=&s"(m0), "=&s"(m1), "=&s"(m2), "=&s"(m3), "+s"(left)
            : "v"(off), "v"(oo), "v"(selr), "s"(a.ws), "s"(a.out), "s"(ta), "s"(tb)
            : "memory", "vcc", "scc", "v64", "v65", "v66", "v67", "v68", "v69", "v70", "v71", "v72", "v73", "v74", "v75", "v76", "v77", "v78", "v79",
              "v80", "v81", "v82", "v83", "v84", "v85");
        if (keeper && lane == 0) __hip_atomic_store((GAS unsigned*)a.ws, tg.epoch + 1u, __ATOMIC_RELAXED, __HIP_MEMORY_SCOPE_AGENT);
      }
    } else if (v == 1) {
      const int r = lane >> 3, c = lane & 7;
      DRAIN_VM();
      lds_signal(L + SIG_TAG, lane);
      {
        const int lc = lane < 56 ? lane : 55;
        const v4u xv = *(LAS v4u*)(L + L_GX + lc * 16), gv = *(LAS v4u*)(L + L_GX + 896 + lc * 16);
        float t = 0.f;
#pragma unroll
        for (int k = 0; k < 4; ++k) { t = fmaf(bflo(xv[k]), bflo(gv[k]), t); t = fmaf(bfhi(xv[k]), bfhi(gv[k]), t); }
        t = wave_sum(lane < 56 ? t : 0.f);
        if (lane == 0) *(LAS float*)(L + L_ST + 32) = 1.f / (1.f + __expf(-t));
      }
      const int g0 = s * PRM(NG), ng = s < 3 ? PRM(NG) : 28 - g0;
      const __amdgpu_buffer_rsrc_t r2 = mkrs(a.w2 + (224 * h + 8 * g0) * (DM * 2), ng * 8 * (DM * 2));
      const int o2 = r * (DM * 2) + c * 16;
      int gi = b % ng;
      lds_await(L + SIG_EP, 1u);
      const Tags tg = make_tags(L + L_EP);
      {
        if ((wg >> 3) == 3 && lane == 0) st_sc1(rs, WS_T + (wg & 7) * 128, v4u{tstart, tg.a, 0u, tg.b});
        not_before(tstart + 250);
        const v4u st = ld_sc1(rs, lane < 8 ? WS_T + lane * 128 : OOB);
        int dt = tags_ok(st, tg.a, tg.b) ? (int)(st.x - tstart) : 0;
        dt += __builtin_amdgcn_mov_dpp(dt, XOR1, 0xf, 0xf, true);
        dt += __builtin_amdgcn_mov_dpp(dt, XOR2, 0xf, 0xf, true);
        dt += __builtin_amdgcn_mov_dpp(dt, HMIR, 0xf, 0xf, true);
        const unsigned origin = tstart + max(-300, min(300, __builtin_amdgcn_readfirstlane(dt) >> 3));
        if (lane == 0) *(volatile LAS unsigned*)(L + L_ST + 48) = origin;
        not_before(origin + PRM(HT));
      }
      const bool mine = (lane & 31) < 2 * ng;
      v4u hg;
#pragma nounroll
      for (unsigned spin = 0; spin < SPIN_CAP; ++spin) {
          CFENCE();
        hg = ld_sc1(rs, mine ? WS_H + (b) * 14336 + (lane >> 5) * 7168 + (56 * h + 2 * g0 + (lane & 31)) * 16 : OOB);
        if (__all(!mine || tags_ok(hg, tg.a, tg.b))) break;
        __builtin_amdgcn_s_sleep(2);
      }
      {
        const float t0 = sw32_add(bflo(hg.x), bflo(hg.z)), t1 = sw32_add(bfhi(hg.x), bfhi(hg.z));
        *(LAS unsigned*)(L + L_HB + (lane & 31) * 8 + (lane >> 5) * 4) = pkbf(t0 / (1.f + __expf(-t0)), t1 / (1.f + __expf(-t1)));
      }
      CFENCE();
      const Sel sel = make_sel(lane);
      const int fdt = (PRM(FT1) - PRM(FT0)) * 64 / (7 * PRM(NG));
      v4u wv[7];
      f32x4 oa[7];
#pragma unroll
      for (int k = 0; k < 7; ++k) {
        oa[k] = f32x4{0.f, 0.f, 0.f, 0.f};
        wv[k] = __builtin_amdgcn_raw_buffer_load_b128(r2, o2 + gi * 8 * (DM * 2) + k * 128, 0, 0);
        SCHED();
      }
#pragma nounroll
      for (int t = 0; t < ng; ++t) {
        const v4u af = place(*(LAS unsigned short*)(L + L_HB + (8 * gi + r) * 2), sel.lo);
        gi = gi + 1 == ng ? 0 : gi + 1;
        const int on = t + 1 < ng ? o2 + gi * 8 * (DM * 2) : OOB - 1024;
#pragma unroll
        for (int k = 0; k < 7; ++k) {
          if (t + 1 < ng) not_before(tstart + PRM(FT0) + (((7 * t + 7 + k) * fdt) >> 6));
          WAIT_VM(6);
          oa[k] = mfma32(af, wv[k], oa[k]);
          wv[k] = __builtin_amdgcn_raw_buffer_load_b128(r2, on + k * 128, 0, 0);
          SCHED();
        }
      }
      if ((lane >> 5) == (r & 1)) {
        LAS char* fo = L + L_FF + ((lane >> 5) * DM + 8 * c + 4 * ((lane >> 4) & 1)) * 4;
#pragma unroll
        for (int k = 0; k < 7; ++k) *(LAS f32x4*)(fo + k * 256) = oa[k];
      }
      lds_signal(L + SIG_W, lane);
    } else {
      if (job) {
        DRAIN_VM();
        lds_signal(L + SIG_TAG, lane);
        lds_await(L + SIG_TAG, 4u);
        LAS char* rp = L + L_WO + ((lane >> 4) * 8 + ((lane >> 2) & 3)) * 128 + v * 32 + (lane & 3) * 8;
        LAS char* xp = L + L_X + ((lane >> 1) & 3) * ROWP + (lane & 1) * 448 + (lane >> 4) * 16;
        v4u xb[7], wa[7], wb[7];
#pragma unroll
        for (int ks = 0; ks < 7; ++ks) {
          xb[ks] = *(LAS v4u*)(xp + ks * 64);
          const v2u l0 = tr_read(rp + ks * 4096), h0 = tr_read(rp + ks * 4096 + 512), l1 = tr_read(rp + ks * 4096 + 32), h1 = tr_read(rp + ks * 4096 + 544);
          wa[ks] = v4u{l0.x, l0.y, h0.x, h0.y}; wb[ks] = v4u{l1.x, l1.y, h1.x, h1.y};
        }
        const Tags tg = make_tags(L + L_EP);
        const int from = ((lane & 3) * 16 + ((lane >> 2) & 1) * 8 + (lane >> 3)) * 4;
        const int dst = (s < 2 ? WS_Q + (hj * 8 + (lane >> 3)) * 4096 + kh * 2048 + b * 256 : WS_H + (lane >> 3) * 14336 + kh * 7168 + (jf >> 1) * 256) + v * 64 + (lane & 7) * 16;
        SCHED();
        f32x4 d0 = {0.f, 0.f, 0.f, 0.f}, d1 = {0.f, 0.f, 0.f, 0.f};
#pragma unroll
        for (int ks = 0; ks < 7; ++ks) { d0 = mfma32(wa[ks], xb[ks], d0); d1 = mfma32(wb[ks], xb[ks], d1); }
        const f32x4 d = (lane & 8) ? d1 : d0;
        const unsigned o0 = __builtin_amdgcn_ds_bpermute(from, pkbf(d[0], d[1])), o1 = __builtin_amdgcn_ds_bpermute(from, pkbf(d[2], d[3]));
        not_before(tstart + PRM(JT) - 60); not_before(clock_origin(L) + PRM(JT));
        st_sc1(rs, dst, v4u{o0, tg.a, o1, tg.b});
        lds_signal(L + SIG_JOB, lane);
        if (v == 0) lds_await(L + SIG_JOB, 2u);
      }
      const GAS char* wop = (const GAS char*)a.wo + (size_t)((2 * h + (v >> 1)) * 256 + 64 * s) * (DM * 2) + lane * 16;
      LAS char* dst = L + L_WO + (v >> 1) * 64 * ROWP;
      if (lane < 56) {
        const int t0 = s == 3 ? PRM(O3T0) : v ? PRM(O2T0) : PRM(OT0), dt = (s == 3 ? PRM(O3T1) : v ? PRM(O2T1) : PRM(OT1)) - t0;
#pragma nounroll
        for (int r = 0; r < 64; ++r) {
                    const int rr = (r + 8 * b) & 63;
          not_before(tstart + t0 + ((r * dt) >> 6)); WAIT_VM(7); dma16(wop + rr * (DM * 2), dst + rr * ROWP);
        }
      }
      DRAIN_VM();
      lds_signal(L + SIG_W, lane);
    }
  }
}

}

extern "C" void kernel_launch(void* const* d_in, const int* in_sizes, int n_in, void* d_out, int out_size, void* d_ws, size_t ws_size,
                              hipStream_t stream) {
  Args a;
  a.x = (const char*)d_in[0];
  a.kc = (const char*)d_in[1];
  a.vc = (const char*)d_in[2];
  a.wq = (const char*)d_in[3];
  a.wo = (const char*)d_in[4];
  a.w1 = (const char*)d_in[6];
  a.w2 = (const char*)d_in[7];
  a.gw = (const char*)d_in[8];
  a.out = (unsigned short*)d_out;
  a.ws = (char*)d_ws;
  fused_decode<<<256, NTHR, 0, stream>>>(a);
}
```
